```python
import torch

def _softmax(x, axis=-1):
    return torch.softmax(x, dim=axis)
def _gelu(x):
    return torch.nn.functional.gelu(x, approximate="tanh")
def _silu(x):
    return torch.nn.functional.silu(x)
def _sigmoid(x):
    return torch.sigmoid(x)
def _layernorm(x, axis=-1, eps=1e-5):
    m = x.mean(dim=axis, keepdim=True)
    v = x.var(dim=axis, keepdim=True, unbiased=False)
    return (x - m) / torch.sqrt(v + eps)
def _scatter_add(tgt, idx, src, axis=0):
    return tgt.clone().index_add_(axis, idx, src)
def _segment_sum(src, seg, n):
    out = torch.zeros((n,) + src.shape[1:], dtype=src.dtype)
    return out.index_add_(0, seg, src)
def _pad(x, pw):
    flat = tuple(v for pair in reversed(pw) for v in pair)
    return torch.nn.functional.pad(x, flat)

kernel_name = 'standalone__low_rank_proj_linformer__prompt'

def setup_inputs():

    _w = (lambda a: (lambda t: t.to(torch.bfloat16) if t.is_floating_point() else t)(torch.as_tensor(a)))
    B, H, Sq, Skv, Dh, R = 1, 32, 2048, 2048, 128, 8
    return {
        "q": _w((lambda *s: torch.randn(*s, dtype=torch.bfloat16) * 0.8)(B, H, Sq, Dh)),
        "k": _w((lambda *s: torch.randn(*s, dtype=torch.bfloat16) * 0.8)(B, H, Skv, Dh)),
        "v": _w((lambda *s: torch.randn(*s, dtype=torch.bfloat16) * 0.05)(B, H, Skv, Dh)),
        "e_proj": _w((lambda *s: torch.randn(*s, dtype=torch.bfloat16) * 0.8)(R, Skv) * (Skv ** -0.5)),
        "f_proj": _w((lambda *s: torch.randn(*s, dtype=torch.bfloat16) * 0.05)(R, Skv) * (Skv ** -0.5)),
    }

def reference(q, k, v, e_proj, f_proj):
    B, H, Sq, Dh = q.shape
    kp = torch.einsum("rn,bhnd->bhrd", e_proj, k)
    vp = torch.einsum("rn,bhnd->bhrd", f_proj, v)
    scores = torch.einsum("bhqd,bhrd->bhqr", q, kp) * (Dh ** -0.5)
    p = _softmax(scores, axis=-1)
    o = torch.einsum("bhqr,bhrd->bhqd", p, vp)
    return o
```

```cpp
#include <hip/hip_runtime.h>
#include <hip/hip_bf16.h>
#include <stdint.h>

typedef __attribute__((ext_vector_type(8))) short s16x8;
typedef __attribute__((ext_vector_type(4))) short s16x4;
typedef __attribute__((ext_vector_type(4))) float f32x4;
typedef __attribute__((ext_vector_type(2))) float f32x2;
typedef __attribute__((ext_vector_type(4))) unsigned u32x4;
typedef __attribute__((ext_vector_type(2))) unsigned u32x2;
typedef __attribute__((ext_vector_type(2))) __bf16 bf16x2;

constexpr int H = 32, S = 2048, D = 128;
constexpr int NSPLIT = 8;
constexpr int ROWS = S / NSPLIT;
constexpr int NW = 8;
constexpr int NT = NW * 64;
constexpr int NWG = H * NSPLIT;
constexpr int A_NWV = 16;

__device__ __forceinline__ u32x4 ld16(const void* p) { return *reinterpret_cast<const u32x4*>(p); }
__device__ __forceinline__ unsigned pk_bf16(float lo, float hi) {
  f32x2 v = {lo, hi};
  return __builtin_bit_cast(unsigned, __builtin_convertvector(v, bf16x2));
}
__device__ __forceinline__ float rnd_bf16(float x) { return __uint_as_float(pk_bf16(x, 0.f) << 16); }

__device__ __forceinline__ void tile_mfma(const u32x4 (&r)[8], s16x8 A, f32x4 (&acc)[8]) {
#pragma unroll
  for (int wd = 0; wd < 4; ++wd) {
    u32x4 lo, hi;
#pragma unroll
    for (int p = 0; p < 4; ++p) {
      lo[p] = __builtin_amdgcn_perm(r[2 * p + 1][wd], r[2 * p][wd], 0x05040100u);
      hi[p] = __builtin_amdgcn_perm(r[2 * p + 1][wd], r[2 * p][wd], 0x07060302u);
    }
    acc[2 * wd] = __builtin_amdgcn_mfma_f32_16x16x32_bf16(A, __builtin_bit_cast(s16x8, lo), acc[2 * wd], 0, 0, 0);
    acc[2 * wd + 1] = __builtin_amdgcn_mfma_f32_16x16x32_bf16(A, __builtin_bit_cast(s16x8, hi), acc[2 * wd + 1], 0, 0, 0);
  }
}

__global__ __launch_bounds__(NT, 2) void linformer_proj(const __hip_bfloat16* __restrict__ k, const __hip_bfloat16* __restrict__ v,
                                                         const __hip_bfloat16* __restrict__ ep, const __hip_bfloat16* __restrict__ fp,
                                                         float* __restrict__ ws) {
  __shared__ __attribute__((aligned(16))) float red[NW][16][128];
  const int tid = threadIdx.x, lane = tid & 63, wave = tid >> 6;
  const int c = lane & 15, g = lane >> 4;
  const int b = blockIdx.x;
  const int h = (b & 7) * 4 + (b >> 6), s = (b >> 3) & 7;
  const int n0 = s * ROWS + wave * 32;
  const size_t base = ((size_t)h * S + n0) * D;
  const u32x4 w = *reinterpret_cast<const u32x4*>((c < 8 ? ep : fp) + (c & 7) * S + n0 + 8 * g);
  u32x4 kr[8], vr[8];
#pragma unroll
  for (int i = 0; i < 8; ++i) kr[i] = ld16(k + base + (size_t)(8 * g + i) * D + 8 * c);
#pragma unroll
  for (int i = 0; i < 8; ++i) vr[i] = ld16(v + base + (size_t)(8 * g + i) * D + 8 * c);
  f32x4 acc[8];
#pragma unroll
  for (int i = 0; i < 8; ++i) acc[i] = (f32x4){0.f, 0.f, 0.f, 0.f};
  const u32x4 z = {0u, 0u, 0u, 0u};
  tile_mfma(kr, __builtin_bit_cast(s16x8, c < 8 ? w : z), acc);
  tile_mfma(vr, __builtin_bit_cast(s16x8, c < 8 ? z : w), acc);
#pragma unroll
  for (int reg = 0; reg < 4; ++reg) {
    f32x4 lo = {acc[0][reg], acc[1][reg], acc[2][reg], acc[3][reg]};
    f32x4 hi = {acc[4][reg], acc[5][reg], acc[6][reg], acc[7][reg]};
    *reinterpret_cast<f32x4*>(&red[wave][4 * g + reg][c * 4]) = lo;
    *reinterpret_cast<f32x4*>(&red[wave][4 * g + reg][64 + c * 4]) = hi;
  }
  __syncthreads();
  const int row = tid >> 5, pos = (tid & 31) * 4;
  f32x4 sum = *reinterpret_cast<const f32x4*>(&red[0][row][pos]);
#pragma unroll
  for (int wv = 1; wv < NW; ++wv) sum += *reinterpret_cast<const f32x4*>(&red[wv][row][pos]);
  const __amdgpu_buffer_rsrc_t rs = __builtin_amdgcn_make_buffer_rsrc(ws, 0, NWG * 8192, 0x00020000);
  __builtin_amdgcn_raw_buffer_store_b128(__builtin_bit_cast(u32x4, sum), rs, (unsigned)((h * NSPLIT + s) * 8192 + tid * 16), 0, 16);
}

struct __attribute__((aligned(16))) Smem2 {
  u32x4 stg[A_NWV][16][16];
  unsigned short kpS[8][136];
  unsigned short vpT[128][8];
};
__global__ __launch_bounds__(A_NWV * 64) void linformer_attn(const __hip_bfloat16* __restrict__ q, __hip_bfloat16* __restrict__ out,
                                                              const float* __restrict__ ws) {
  __shared__ Smem2 sm;
  constexpr int RW = A_NWV * 16, WPH = S / RW, NB = H * WPH;
  const int tid = threadIdx.x, lane = tid & 63, wave = tid >> 6;
  const int m = lane & 15, g = lane >> 4;
  const int b = (blockIdx.x & 7) * (NB / 8) + (blockIdx.x >> 3);
  const int h = b / WPH;
  const size_t base = ((size_t)b * RW + wave * 16) * D;
  f32x4 pp[NSPLIT];
  if (tid < 512) {
#pragma unroll
    for (int j = 0; j < NSPLIT; ++j) pp[j] = *reinterpret_cast<const f32x4*>(ws + (size_t)(h * NSPLIT + j) * 2048 + tid * 4);
  }
  __builtin_amdgcn_s_barrier();
  u32x4 qr[4];
#pragma unroll
  for (int i = 0; i < 4; ++i) qr[i] = ld16(q + base + (size_t)(4 * i + g) * D + 8 * m);
  if (tid < 512) {
    f32x4 tot = pp[0];
#pragma unroll
    for (int j = 1; j < NSPLIT; ++j) tot += pp[j];
    const int row = tid >> 5, d0 = 8 * (tid & 15) + 4 * ((tid >> 4) & 1);
    const unsigned p01 = pk_bf16(tot[0], tot[1]), p23 = pk_bf16(tot[2], tot[3]);
    if (row < 8) {
      u32x2 v = {p01, p23};
      *reinterpret_cast<u32x2*>(&sm.kpS[row][d0]) = v;
    } else {
      sm.vpT[d0 + 0][row - 8] = (unsigned short)(p01 & 0xffffu);
      sm.vpT[d0 + 1][row - 8] = (unsigned short)(p01 >> 16);
      sm.vpT[d0 + 2][row - 8] = (unsigned short)(p23 & 0xffffu);
      sm.vpT[d0 + 3][row - 8] = (unsigned short)(p23 >> 16);
    }
  }
  __syncthreads();
  s16x8 kA[4];
#pragma unroll
  for (int st = 0; st < 4; ++st) {
    u32x4 v = *reinterpret_cast<const u32x4*>(&sm.kpS[m & 7][32 * st + 8 * g]);
    const u32x4 zz = {0u, 0u, 0u, 0u};
    kA[st] = __builtin_bit_cast(s16x8, m < 8 ? v : zz);
  }
  s16x4 vA[8];
#pragma unroll
  for (int t = 0; t < 8; ++t) {
    const int d = 32 * (t >> 1) + 8 * (m >> 2) + 4 * (t & 1) + (m & 3);
    u32x2 v = *reinterpret_cast<const u32x2*>(&sm.vpT[d][4 * (g & 1)]);
    const u32x2 zz = {0u, 0u};
    vA[t] = __builtin_bit_cast(s16x4, g < 2 ? v : zz);
  }
#pragma unroll
  for (int i = 0; i < 4; ++i) { const int r = 4 * i + g; sm.stg[wave][r][m ^ r] = qr[i]; }
  __builtin_amdgcn_wave_barrier();
  u32x4 qf[4];
#pragma unroll
  for (int st = 0; st < 4; ++st) qf[st] = sm.stg[wave][m][(4 * st + g) ^ m];
  f32x4 sc = {0.f, 0.f, 0.f, 0.f};
#pragma unroll
  for (int st = 0; st < 4; ++st)
    sc = __builtin_amdgcn_mfma_f32_16x16x32_bf16(kA[st], __builtin_bit_cast(s16x8, qf[st]), sc, 0, 0, 0);
  float x[4];
#pragma unroll
  for (int j = 0; j < 4; ++j) x[j] = rnd_bf16(rnd_bf16(sc[j]) * 0.08838834764831845f);
  float mx = fmaxf(fmaxf(x[0], x[1]), fmaxf(x[2], x[3]));
  {
    auto sw = __builtin_amdgcn_permlane16_swap(__float_as_uint(mx), __float_as_uint(mx), false, false);
    mx = fmaxf(__uint_as_float(sw[0]), __uint_as_float(sw[1]));
  }
  float e[4], sum = 0.f;
#pragma unroll
  for (int j = 0; j < 4; ++j) { e[j] = __expf(x[j] - mx); sum += e[j]; }
  {
    auto sw = __builtin_amdgcn_permlane16_swap(__float_as_uint(sum), __float_as_uint(sum), false, false);
    sum = __uint_as_float(sw[0]) + __uint_as_float(sw[1]);
  }
  const float inv = 1.0f / sum;
  u32x2 pb = {pk_bf16(e[0] * inv, e[1] * inv), pk_bf16(e[2] * inv, e[3] * inv)};
  if (g >= 2) { pb[0] = 0u; pb[1] = 0u; }
  const s16x4 pB = __builtin_bit_cast(s16x4, pb);
  __builtin_amdgcn_wave_barrier();
#pragma unroll
  for (int i = 0; i < 4; ++i) {
    const f32x4 zc = {0.f, 0.f, 0.f, 0.f};
    f32x4 c0 = __builtin_amdgcn_mfma_f32_16x16x16bf16_1k(vA[2 * i], pB, zc, 0, 0, 0);
    f32x4 c1 = __builtin_amdgcn_mfma_f32_16x16x16bf16_1k(vA[2 * i + 1], pB, zc, 0, 0, 0);
    u32x4 o = {pk_bf16(c0[0], c0[1]), pk_bf16(c0[2], c0[3]), pk_bf16(c1[0], c1[1]), pk_bf16(c1[2], c1[3])};
    sm.stg[wave][m][(4 * i + g) ^ m] = o;
  }
  __builtin_amdgcn_wave_barrier();
  const __amdgpu_buffer_rsrc_t ro = __builtin_amdgcn_make_buffer_rsrc(out, 0, H * S * D * 2, 0x00020000);
#pragma unroll
  for (int i = 0; i < 4; ++i) {
    const int r = 4 * i + g;
    __builtin_amdgcn_raw_buffer_store_b128(sm.stg[wave][r][m ^ r], ro, (unsigned)((base + (size_t)r * D + 8 * m) * 2), 0, 16);
  }
}

extern "C" void kernel_launch(void* const* d_in, const int* in_sizes, int n_in, void* d_out, int out_size, void* d_ws,
                              size_t ws_size, hipStream_t stream) {
  const __hip_bfloat16* q = (const __hip_bfloat16*)d_in[0];
  const __hip_bfloat16* k = (const __hip_bfloat16*)d_in[1];
  const __hip_bfloat16* v = (const __hip_bfloat16*)d_in[2];
  const __hip_bfloat16* ep = (const __hip_bfloat16*)d_in[3];
  const __hip_bfloat16* fp = (const __hip_bfloat16*)d_in[4];
  __hip_bfloat16* out = (__hip_bfloat16*)d_out;
  float* ws = (float*)d_ws;
  linformer_proj<<<NWG, NT, 0, stream>>>(k, v, ep, fp, ws);
  linformer_attn<<<H * S / (A_NWV * 16), A_NWV * 64, 0, stream>>>(q, out, ws);
}
```
